```python
import torch

def _softmax(x, axis=-1):
    return torch.softmax(x, dim=axis)

def _gelu(x):
    return torch.nn.functional.gelu(x, approximate='tanh')

def _silu(x):
    return torch.nn.functional.silu(x)

def _sigmoid(x):
    return torch.sigmoid(x)

def _layernorm(x, axis=-1, eps=1e-05):
    m = x.mean(dim=axis, keepdim=True)
    v = x.var(dim=axis, keepdim=True, unbiased=False)
    return (x - m) / torch.sqrt(v + eps)

def _scatter_add(tgt, idx, src, axis=0):
    return tgt.clone().index_add_(axis, idx, src)

def _segment_sum(src, seg, n):
    out = torch.zeros((n,) + src.shape[1:], dtype=src.dtype)
    return out.index_add_(0, seg, src)

def _pad(x, pw):
    flat = tuple((v for pair in reversed(pw) for v in pair))
    return torch.nn.functional.pad(x, flat)
kernel_name = 'adaLN_cond__mha_block_cached__dense_activation_silu__decode'

def setup_inputs():

    _w = lambda a: (lambda t: t.to(torch.bfloat16) if t.is_floating_point() else t)(torch.as_tensor(a))

    def _A_setup():
        B, Sq, D = (8, 1, 640)
        H, Sk, Dh = (32, 4096, 128)
        return {'x': _w((lambda *s: torch.randn(*s, dtype=torch.bfloat16) * 0.1)(B, Sq, D)), 'k_cache': _w((lambda *s: torch.randn(*s, dtype=torch.bfloat16) * 0.8)(B, H, Sk, Dh)), 'v_cache': _w((lambda *s: torch.randn(*s, dtype=torch.bfloat16) * 3.2)(B, H, Sk, Dh)), 'wq': _w((lambda *s: torch.randn(*s, dtype=torch.bfloat16) * 0.8)(D, H * Dh) * D ** (-0.5)), 'wo': _w((lambda *s: torch.randn(*s, dtype=torch.bfloat16) * 3.2)(H * Dh, D) * (H * Dh) ** (-0.5))}
    _A_in = _A_setup()

    def _B_setup():
        B, S, D, F = (8, 1, 640, 1728)
        return {'x': _w((lambda *s: torch.randn(*s, dtype=torch.bfloat16) * 0.1)(B, S, D)), 'w1': _w((lambda *s: torch.randn(*s, dtype=torch.bfloat16) * 3.2)(D, F) * D ** (-0.5)), 'w2': _w((lambda *s: torch.randn(*s, dtype=torch.bfloat16) * 3.2)(F, D) * F ** (-0.5))}
    _B_in = _B_setup()
    return {'A_x': _A_in['x'], 'A_k_cache': _A_in['k_cache'], 'A_v_cache': _A_in['v_cache'], 'A_wq': _A_in['wq'], 'A_wo': _A_in['wo'], 'B_x': _B_in['x'], 'B_w1': _B_in['w1'], 'B_w2': _B_in['w2'], 'c': _w((lambda *s: torch.randn(*s, dtype=torch.bfloat16) * 0.2)(8, 640) * 8.0), 'w_mod': _w((lambda *s: torch.randn(*s, dtype=torch.bfloat16) * 0.2)(640, 6 * 640) * 640 ** (-0.5) * 8.0)}

def reference(A_x, A_k_cache, A_v_cache, A_wq, A_wo, B_x, B_w1, B_w2, c, w_mod):

    def _A_ref(x, k_cache, v_cache, wq, wo):
        B, Sq, D = x.shape
        H, Dh = (32, 128)
        q = (lambda x, *d: x.permute(*d))((x @ wq).reshape(B, Sq, H, Dh), 0, 2, 1, 3)
        scores = torch.einsum('bhqd,bhkd->bhqk', q, k_cache) * Dh ** (-0.5)
        p = _softmax(scores, axis=-1)
        o = torch.einsum('bhqk,bhkd->bhqd', p, v_cache)
        return (lambda x, *d: x.permute(*d))(o, 0, 2, 1, 3).reshape(B, Sq, H * Dh) @ wo

    def _B_ref(x, w1, w2):
        h = torch.einsum('bsd,df->bsf', x, w1)
        h = _silu(h)
        out = torch.einsum('bsf,fd->bsd', h, w2)
        return out
    x = A_x
    mod = (_silu(c) @ w_mod).reshape(c.shape[0], 6, 1, -1)
    sa, ca, ga, sb, cb, gb = (mod[:, 0].clone(), mod[:, 1].clone(), mod[:, 2].clone(), mod[:, 3].clone(), mod[:, 4].clone(), mod[:, 5].clone())
    h = _layernorm(x, axis=-1) * (1 + ca) + sa
    x = x + ga * _A_ref(h, A_k_cache, A_v_cache, A_wq, A_wo)
    h = _layernorm(x, axis=-1) * (1 + cb) + sb
    x = x + gb * _B_ref(h, B_w1, B_w2)
    return x
```

```cpp
#define N0A 1
#define N0B 2
#include <hip/hip_runtime.h>
#include <hip/hip_bf16.h>
#include <cstdint>

typedef unsigned int u32;
typedef unsigned long long u64;
typedef unsigned short u16;
using u32x4 = __attribute__((ext_vector_type(4))) unsigned;
using f32x4 = __attribute__((ext_vector_type(4))) float;
using f32x2 = __attribute__((ext_vector_type(2))) float;
using bf16x2 = __attribute__((ext_vector_type(2))) __bf16;
using bf16x8 = __attribute__((ext_vector_type(8))) __bf16;
using u16x8 = __attribute__((ext_vector_type(8))) unsigned short;

#define NB 3
#define ROT 0
#define PACE 4
#define N1A 2
#define N1B 5
#define N2A 2
#define HMAP 0
#define ABAR 1
#define WOS 2
#define CPF 0
#define LOCKSTEP 1
#define DBG_TS 0

constexpr int B_ = 8, D_ = 640, H_ = 32, SK = 4096, DH = 128, F_ = 1728, NMOD = 3840;
constexpr int NWG = 256, NTHR = 512, NT = 30;
constexpr size_t G_EPOCH = 0, G_READY = 512, G_MOD = 1024, G_P1 = G_MOD + (size_t)B_ * NMOD, G_X1 = G_P1 + (size_t)B_ * 20 * 32 * 32,
                 G_P3 = G_X1 + (size_t)B_ * D_, G_Q = G_P3 + (size_t)B_ * 20 * 32 * 32, G_END = G_Q + (size_t)NWG * 8 * DH, G_TS = G_END + 512;
constexpr int L_W1 = 0;
constexpr int L_W2 = 71680;
constexpr int L_RED = L_W2 + 71680;
constexpr int L_MRG = L_RED + 10240;
constexpr int L_HV = L_MRG + 8 * 132 * 4;
constexpr int L_PV = L_HV + 1280;
constexpr int L_SC = L_PV + 1024;
constexpr int L_END = L_SC + 2560 + 512;
static_assert(L_END <= 163840, "LDS");
constexpr unsigned SPIN_CAP = 1u << 20;

#define RLX_AGENT __ATOMIC_RELAXED, __HIP_MEMORY_SCOPE_AGENT
__device__ __forceinline__ float bf2f(u32 bits16) { return __uint_as_float(bits16 << 16); }
__device__ __forceinline__ float bfr(float x) { return (float)(__bf16)x; }
__device__ __forceinline__ u32 pk2(float lo, float hi) { bf16x2 v = {(__bf16)lo, (__bf16)hi}; return __builtin_bit_cast(u32, v); }
__device__ __forceinline__ float dot2(u32 a, u32 b, float acc) {
  return __builtin_amdgcn_fdot2_f32_bf16(__builtin_bit_cast(bf16x2, a), __builtin_bit_cast(bf16x2, b), acc, false);
}
template <int CTRL> __device__ __forceinline__ float dpp(float x) {
  return __builtin_bit_cast(float, __builtin_amdgcn_mov_dpp(__builtin_bit_cast(int, x), CTRL, 0xf, 0xf, true));
}
constexpr int ROR1 = 0x121, ROR2 = 0x122, ROR4 = 0x124, ROR8 = 0x128;
__device__ __forceinline__ float row_sum(float x) { x += dpp<ROR8>(x); x += dpp<ROR4>(x); x += dpp<ROR2>(x); x += dpp<ROR1>(x); return x; }
__device__ __forceinline__ float tr32(float a, float b) {
  auto s = __builtin_amdgcn_permlane32_swap(__float_as_uint(a), __float_as_uint(b), false, false);
  return __uint_as_float(s[0]) + __uint_as_float(s[1]);
}
__device__ __forceinline__ float tr16(float a, float b) {
  auto s = __builtin_amdgcn_permlane16_swap(__float_as_uint(a), __float_as_uint(b), false, false);
  return __uint_as_float(s[0]) + __uint_as_float(s[1]);
}
__device__ __forceinline__ float xrow_sum(float x) { x = tr16(x, x); return tr32(x, x); }
__device__ __forceinline__ float xrow_max(float x) {
  auto s = __builtin_amdgcn_permlane16_swap(__float_as_uint(x), __float_as_uint(x), false, false);
  x = fmaxf(__uint_as_float(s[0]), __uint_as_float(s[1]));
  auto t = __builtin_amdgcn_permlane32_swap(__float_as_uint(x), __float_as_uint(x), false, false);
  return fmaxf(__uint_as_float(t[0]), __uint_as_float(t[1]));
}
__device__ __forceinline__ float wave_sum(float x) { return xrow_sum(row_sum(x)); }
__device__ __forceinline__ void lds_barrier() { asm volatile("s_waitcnt lgkmcnt(0)\n\ts_barrier" ::: "memory"); }
__device__ __forceinline__ float silu_f(float x) { return x * __builtin_amdgcn_rcpf(1.f + __builtin_amdgcn_exp2f(-1.4426950408889634f * x)); }

struct Tag { u32 lo, hi; };
__device__ __forceinline__ u64 mk_gran(float v, Tag t) {
  u32 u = __float_as_uint(v);
  u32 p = ((u & 0x7f800000u) == 0x7f800000u) ? (u | ((u & 0xffu) ? 0x100u : 0u)) : (u + 0x80u);
  return ((u64)((p & 0xffffff00u) | t.hi) << 32) | t.lo;
}
__device__ __forceinline__ void put(u64* g, float v, Tag t) { __hip_atomic_store(g, mk_gran(v, t), RLX_AGENT); }
template <int N, int SPLIT = N> __device__ __forceinline__ void sweep(const u64* g, int S, Tag t, float (&v)[N], int real = 1) {
  for (unsigned spins = 0;; ++spins) {
    bool ok = true;
#pragma unroll
    for (int k = 0; k < N; ++k) {
      const u64 x = __hip_atomic_load(g + (k < SPLIT ? (size_t)k * S : (size_t)(k - SPLIT) * S + D_), RLX_AGENT);
      const u32 hi = (u32)(x >> 32);
      v[k] = __uint_as_float(hi & 0xffffff00u);
      ok &= ((u32)x == t.lo) & ((hi & 0xffu) == t.hi);
    }
    if (__all(ok) || spins > SPIN_CAP || !real) return;
    __builtin_amdgcn_s_sleep(1);
  }
}
__device__ __forceinline__ void glds16(const void* gsrc, u32 lds_dst) {
  u32 keep;
  asm volatile("s_mov_b32 %0, m0\n\ts_mov_b32 m0, %2\n\ts_nop 0\n\tglobal_load_lds_dwordx4 %1, off\n\ts_mov_b32 m0, %0" : "=&s"(keep) : "v"(gsrc), "s"(lds_dst) : "memory");
}
__device__ __forceinline__ u32x4 ldnt(const void* p) { return __builtin_nontemporal_load(reinterpret_cast<const u32x4*>(p)); }
__device__ __forceinline__ u32x4 ld16(const void* p) { return *reinterpret_cast<const u32x4*>(p); }
using u32x2 = __attribute__((ext_vector_type(2))) unsigned;
using s16x4 = __attribute__((ext_vector_type(4))) short;
__device__ __forceinline__ u32 plo(u32 r0, u32 r1) { return __builtin_amdgcn_perm(r1, r0, 0x05040100u); }
__device__ __forceinline__ u32 phi(u32 r0, u32 r1) { return __builtin_amdgcn_perm(r1, r0, 0x07060302u); }
__device__ __forceinline__ void tr8x4(const u32x2 (&R)[8], u32x4* F) {
#pragma unroll
  for (int k = 0; k < 4; ++k) {
    F[0][k] = plo(R[2 * k][0], R[2 * k + 1][0]); F[1][k] = phi(R[2 * k][0], R[2 * k + 1][0]);
    F[2][k] = plo(R[2 * k][1], R[2 * k + 1][1]); F[3][k] = phi(R[2 * k][1], R[2 * k + 1][1]);
  }
}
__device__ __forceinline__ f32x4 mfma32(const u32x4& a, const u32x4& b, const f32x4& c) {
  return __builtin_amdgcn_mfma_f32_16x16x32_bf16(__builtin_bit_cast(bf16x8, a), __builtin_bit_cast(bf16x8, b), c, 0, 0, 0);
}
__device__ __forceinline__ f32x4 mfma16(const u32x2& a, const u32x2& b, const f32x4& c) {
  return __builtin_amdgcn_mfma_f32_16x16x16bf16_1k(__builtin_bit_cast(s16x4, a), __builtin_bit_cast(s16x4, b), c, 0, 0, 0);
}
__device__ __forceinline__ float sel4(int g, float a, float b, float c, float d) { return (g & 2) ? ((g & 1) ? d : c) : ((g & 1) ? b : a); }
__device__ __forceinline__ void lds_wait() { asm volatile("s_waitcnt lgkmcnt(0)" ::: "memory"); }
__device__ __forceinline__ void ln_stats(const float (&v)[10], float& mb, float& den) {
  float s = 0.f;
#pragma unroll
  for (int i = 0; i < 10; ++i) s += v[i];
  const float mean = wave_sum(s) * (1.f / 640.f);
  float q = 0.f;
#pragma unroll
  for (int i = 0; i < 10; ++i) { const float d = v[i] - mean; q = fmaf(d, d, q); }
  const float var = bfr(wave_sum(q) * (1.f / 640.f));
  mb = bfr(mean);
  den = bfr(sqrtf(bfr(var + 1e-5f)));
}
__device__ __forceinline__ void ln_mod(const float (&v)[10], const float (&sh)[10], const float (&sc)[10], u16* hv, int lane) {
  float mb, den;
  ln_stats(v, mb, den);
#pragma unroll
  for (int i = 0; i < 10; ++i) {
    const float ln = bfr(bfr(v[i] - mb) / den);
    const float hh = bfr(ln * bfr(1.f + sc[i])) + sh[i];
    hv[lane + 64 * i] = __builtin_bit_cast(u16, (__bf16)hh);
  }
}
struct Args {
  const u16 *x, *k, *v, *wq, *wo, *w1, *w2, *c, *wmod;
  u16* out;
  u64* ws;
};

#define TS(n) do {} while (0)

struct Attn { float m, l, acc[8]; };
__device__ __forceinline__ void ld4nt(u32x4 (&R)[4], u32 voff, const void* sbase) {
  asm volatile("s_nop 4\n\tglobal_load_dwordx4 %0, %4, %5 nt\n\tglobal_load_dwordx4 %1, %4, %5 offset:1024 nt\n\t"
               "global_load_dwordx4 %2, %4, %5 offset:2048 nt\n\tglobal_load_dwordx4 %3, %4, %5 offset:3072 nt"
               : "=&v"(R[0]), "=&v"(R[1]), "=&v"(R[2]), "=&v"(R[3]) : "v"(voff), "s"(sbase));
}
__device__ __forceinline__ void re4nt(u32x4 (&R)[4], u32 voff, const void* sbase) {
  asm volatile("s_nop 4\n\tglobal_load_dwordx4 %0, %4, %5 nt\n\tglobal_load_dwordx4 %1, %4, %5 offset:1024 nt\n\t"
               "global_load_dwordx4 %2, %4, %5 offset:2048 nt\n\tglobal_load_dwordx4 %3, %4, %5 offset:3072 nt"
               : "+v"(R[0]), "+v"(R[1]), "+v"(R[2]), "+v"(R[3]) : "v"(voff), "s"(sbase));
}
__device__ __forceinline__ void re4nt_if(int cond, u32x4 (&R)[4], u32 voff, const void* sbase) {
  asm volatile("s_cmp_eq_u32 %6, 0\n\ts_cbranch_scc1 1f\n\ts_nop 4\n\tglobal_load_dwordx4 %0, %4, %5 nt\n\tglobal_load_dwordx4 %1, %4, %5 offset:1024 nt\n\t"
               "global_load_dwordx4 %2, %4, %5 offset:2048 nt\n\tglobal_load_dwordx4 %3, %4, %5 offset:3072 nt\n1:"
               : "+v"(R[0]), "+v"(R[1]), "+v"(R[2]), "+v"(R[3]) : "v"(voff), "s"(sbase), "s"(cond) : "scc");
}
__device__ __forceinline__ void ldw(u32x4& R, u32 voff, const void* sbase) {
  asm volatile("s_nop 4\n\tglobal_load_dwordx4 %0, %1, %2" : "=&v"(R) : "v"(voff), "s"(sbase));
}
__device__ __forceinline__ void ldw2(u32x2& R, u32 voff, const void* sbase) {
  asm volatile("s_nop 4\n\tglobal_load_dwordx2 %0, %1, %2" : "=&v"(R) : "v"(voff), "s"(sbase));
}
__device__ __forceinline__ void ldh(u32& R, u32 voff, const void* sbase) {
  asm volatile("s_nop 4\n\tglobal_load_ushort %0, %1, %2" : "=&v"(R) : "v"(voff), "s"(sbase));
}
template <int N> __device__ __forceinline__ void wait4(u32x4 (&R)[4]) {
  asm volatile("s_waitcnt vmcnt(%4)" : "+v"(R[0]), "+v"(R[1]), "+v"(R[2]), "+v"(R[3]) : "n"(N));
}
__device__ __forceinline__ void tile_qk(Attn& st, const u32x4 (&K)[4], const u32x4& q, float (&p)[4]) {
  float s[4];
#pragma unroll
  for (int i = 0; i < 4; ++i) {
    float d = dot2(K[i][0], q[0], 0.f);
#pragma unroll
    for (int k = 1; k < 4; ++k) d = dot2(K[i][k], q[k], d);
    d = row_sum(d);
    s[i] = bfr(bfr(d) * 0.08838834764831845f) * 1.4426950408889634f;
  }
  const float mn = fmaxf(fmaxf(fmaxf(s[0], s[1]), fmaxf(s[2], s[3])), st.m);
  const float alpha = __builtin_amdgcn_exp2f(st.m - mn);
  st.m = mn;
#pragma unroll
  for (int i = 0; i < 4; ++i) p[i] = __builtin_amdgcn_exp2f(s[i] - mn);
  st.l = fmaf(st.l, alpha, (p[0] + p[1]) + (p[2] + p[3]));
#pragma unroll
  for (int k = 0; k < 8; ++k) st.acc[k] *= alpha;
}
__device__ __forceinline__ void tile_pv(Attn& st, const u32x4 (&V)[4], const float (&p)[4]) {
#pragma unroll
  for (int i = 0; i < 4; ++i)
#pragma unroll
    for (int k = 0; k < 4; ++k) {
      st.acc[2 * k] = fmaf(p[i], __uint_as_float(V[i][k] << 16), st.acc[2 * k]);
      st.acc[2 * k + 1] = fmaf(p[i], __uint_as_float(V[i][k] & 0xffff0000u), st.acc[2 * k + 1]);
    }
}

template <int N> __device__ __forceinline__ void vmwait() { asm volatile("s_waitcnt vmcnt(%0)" :: "n"(N)); }

__global__ void __launch_bounds__(NTHR, 2) fused_block(Args a) {
  extern __shared__ __attribute__((aligned(16))) unsigned char lds[];
  int tid = threadIdx.x;
  const int w = __builtin_amdgcn_readfirstlane(tid >> 6);
  if (w < 5) __builtin_amdgcn_s_setprio(3);
  const int wg = blockIdx.x, xcd = wg & 7, rr = wg >> 3;
  const int h = HMAP == 0 ? xcd + 8 * (rr & 3) : HMAP == 1 ? 4 * xcd + (rr & 3) : 16 * (xcd & 1) + (rr & 15), b = HMAP < 2 ? rr >> 2 : 2 * (xcd >> 1) + (rr >> 4);
  const int g = (rr & 3) + 4 * (xcd + 8 * (rr >> 2));
  TS(0);
  const unsigned char* kb_ = reinterpret_cast<const unsigned char*>(a.k) + ((size_t)(b * H_ + h) << 20);
  const unsigned char* vb_ = reinterpret_cast<const unsigned char*>(a.v) + ((size_t)(b * H_ + h) << 20);
  const int rot = (ROT * wg) & 255;
  const int half = w >> 2, wq4 = w & 3, hr = min(h, 19);
  const int fg0 = min((27 * h) >> 2, 209), f0 = 8 * fg0;
  const int own0 = 8 * ((27 * h) >> 2), own1 = 8 * ((27 * (h + 1)) >> 2);
  u16* scb = reinterpret_cast<u16*>(lds + L_SC);
  float* scf = reinterpret_cast<float*>(lds + L_SC + 2560);
  u32x4 Kb[NB][4], Vb[NB][4], q;
  u32x2 Wo[5][8];
  Attn st;
  Tag tag;
#define UNDEF(x) asm volatile("" : "=v"(x))
#define TOFF(it_) ((u32)(((w + 8 * (it_) + rot) & 255) << 12) + lane * 16)
#define STEP(s_, it_, REM) do { float p_[4]; const u32 off_ = TOFF((it_) + NB); \
    wait4<4 + 8 * ((REM) < NB ? (REM) : NB - 1)>(Kb[s_]); tile_qk(st, Kb[s_], q, p_); \
    asm volatile("" : "+v"(p_[0]), "+v"(p_[1]), "+v"(p_[2]), "+v"(p_[3])); if ((REM) >= NB) re4nt(Kb[s_], off_, kb_); \
    wait4<((REM) >= NB ? 8 * (NB - 1) + 4 : 8 * (REM))>(Vb[s_]); tile_pv(st, Vb[s_], p_); \
    asm volatile("" : "+v"(st.acc[0]), "+v"(st.acc[1]), "+v"(st.acc[2]), "+v"(st.acc[3]), "+v"(st.acc[4]), "+v"(st.acc[5]), "+v"(st.acc[6]), "+v"(st.acc[7])); \
    if ((REM) >= NB) re4nt(Vb[s_], off_, vb_); } while (0)
  static_assert(NB == 3, "prologue issue order is written for NB = 3");
  constexpr int real = 1;
  {
#pragma unroll
    for (int s = 0; s < NB; ++s)
#pragma unroll
      for (int i = 0; i < 4; ++i) { UNDEF(Kb[s][i]); UNDEF(Vb[s][i]); }
    {
      const int lane = tid & 63;
      float* red = reinterpret_cast<float*>(lds + L_RED);
      u64 E1;
      asm volatile("s_nop 4\n\tglobal_load_dwordx2 %0, %1, %2 sc1" : "=&v"(E1) : "v"(0u), "s"(a.ws + G_EPOCH));
      u32 xs[10], wf[5][4]; u32x2 cf[5], xf; u32x4 W[4];
      u32x4 XK[2][4], XV[2][4];
#pragma unroll
      for (int i = 0; i < 8; ++i) { UNDEF(XK[i >> 2][i & 3]); UNDEF(XV[i >> 2][i & 3]); }
      int e0 = (w + 3) >> 3;
      asm volatile("" : "+s"(e0));
      const int n0 = N0A + e0 * (N0B - N0A), n1 = N1A + e0 * (N1B - N1A), n2 = N2A + ((w + 6) >> 3) * (5 - N2A);
#define ISSUE(lo_, hi_) do { int m_ = ((1 << (hi_)) - 1) & ~((1 << (lo_)) - 1); asm volatile("" : "+s"(m_)); \
      re4nt_if(m_ & 1, XK[0], TOFF(NT), kb_); re4nt_if(m_ & 1, XV[0], TOFF(NT), vb_); re4nt_if(m_ & 2, XK[1], TOFF(NT + 1), kb_); re4nt_if(m_ & 2, XV[1], TOFF(NT + 1), vb_); \
      re4nt_if(m_ & 4, Kb[0], TOFF(0), kb_); re4nt_if(m_ & 4, Vb[0], TOFF(0), vb_); re4nt_if(m_ & 8, Kb[1], TOFF(1), kb_); re4nt_if(m_ & 8, Vb[1], TOFF(1), vb_); \
      re4nt_if(m_ & 16, Kb[2], TOFF(2), kb_); re4nt_if(m_ & 16, Vb[2], TOFF(2), vb_); } while (0)
      const int k0 = 80 * b + 16 * min(w, 4) + 4 * (lane >> 4);
      {
#pragma unroll
        for (int i = 0; i < 10; ++i) ldh(xs[i], (u32)(lane + 64 * i) * 2u, a.x + w * D_);
        const int gc = g < 240 ? g : 0;
        const u32 vo = (u32)(4 * (lane >> 4) * NMOD + 16 * gc + (lane & 15)) * 2u, vc = (u32)((lane & 7) * D_ + 4 * (lane >> 4)) * 2u;
#pragma unroll
        for (int i = 0; i < 5; ++i) ldw2(cf[i], vc, a.c + (w + 8 * i) * 16);
#pragma unroll
        for (int i = 0; i < 5; ++i) {
          const u16* p = a.wmod + (size_t)(w + 8 * i) * 16 * NMOD;
#pragma unroll
          for (int j = 0; j < 4; ++j) ldh(wf[i][j], vo + j * (NMOD * 2), p);
        }
        ldw2(xf, (u32)((lane & 7) * D_ + k0) * 2u, a.x);
#pragma unroll
        for (int j = 0; j < 4; ++j) ldw(W[j], (u32)((k0 + j) * (H_ * DH) + h * DH + 8 * (lane & 15)) * 2u, a.wq);
      }
      if (ABAR) __builtin_amdgcn_s_barrier();
      ISSUE(0, n0);
      static_assert(N0A <= N0B && N0B <= 4 && N0A <= N1A && N0B <= N1B && N1A <= N2A, "issue plan");
#define WAITC(n) asm volatile("s_waitcnt vmcnt(%1)\n\ts_cmp_lg_u32 %0, 0\n\ts_cbranch_scc1 1f\n\ts_waitcnt vmcnt(%2)\n1:" :: "s"(e0), "n"((n) + 8 * N0B), "n"((n) + 8 * N0A) : "scc")
      WAITC(25);
      asm volatile("" : "+v"(cf[0]), "+v"(cf[1]), "+v"(cf[2]), "+v"(cf[3]), "+v"(cf[4]), "+v"(E1));
      asm volatile("" : "+v"(xs[0]), "+v"(xs[1]), "+v"(xs[2]), "+v"(xs[3]), "+v"(xs[4]), "+v"(xs[5]), "+v"(xs[6]), "+v"(xs[7]), "+v"(xs[8]), "+v"(xs[9]));
#pragma unroll
      for (int i = 0; i < 5; ++i)
#pragma unroll
        for (int k = 0; k < 2; ++k) cf[i][k] = pk2(silu_f(__uint_as_float(cf[i][k] << 16)), silu_f(__uint_as_float(cf[i][k] & 0xffff0000u)));
      E1 += 1ull;
      const u64 T40 = E1 * 0x9E3779B97F4A7C15ull;
      tag.lo = (u32)T40; tag.hi = (u32)(T40 >> 32) & 0xffu;
      {
        float xv[10], mb, den;
#pragma unroll
        for (int i = 0; i < 10; ++i) xv[i] = bf2f(xs[i]);
        ln_stats(xv, mb, den);
        if (lane == 0) *reinterpret_cast<f32x2*>(red + 2048 + 2 * w) = f32x2{mb, den};
      }
      asm volatile("" : "+v"(cf[0]), "+v"(cf[1]), "+v"(cf[2]), "+v"(cf[3]), "+v"(cf[4]));
      WAITC(5);
      TS(11);
      {
        f32x4 acc = {0.f, 0.f, 0.f, 0.f};
#pragma unroll
        for (int i = 0; i < 5; ++i) {
          asm volatile("" : "+v"(wf[i][0]), "+v"(wf[i][1]), "+v"(wf[i][2]), "+v"(wf[i][3]));
          acc = mfma16(u32x2{wf[i][0] | (wf[i][1] << 16), wf[i][2] | (wf[i][3] << 16)}, cf[i], acc);
        }
        if ((lane & 15) < 8) *reinterpret_cast<f32x4*>(red + w * 128 + (lane & 15) * 16 + 4 * (lane >> 4)) = acc;
      }
      lds_barrier();
      if (tid < 128 && g < 240) {
        const float s = ((red[tid] + red[128 + tid]) + (red[256 + tid] + red[384 + tid])) + ((red[512 + tid] + red[640 + tid]) + (red[768 + tid] + red[896 + tid]));
        put(a.ws + G_MOD + (size_t)(tid >> 4) * NMOD + 16 * g + (tid & 15), bfr(s), tag);
      }
      if (tid == 128) put(a.ws + G_READY + wg, 0.f, tag);
      TS(1);
      WAITC(0);
      asm volatile("" : "+v"(W[0]), "+v"(W[1]), "+v"(W[2]), "+v"(W[3]), "+v"(xf));
      float* qp = reinterpret_cast<float*>(lds + L_W1);
      ISSUE(n0, n1);
      if (w < 5) {
        float ln[4], sm[4], hh[4];
        const f32x2 sd = *reinterpret_cast<const f32x2*>(red + 2048 + 2 * (lane & 7));
#pragma unroll
        for (int j = 0; j < 4; ++j) ln[j] = bfr(bfr(((j & 1) ? __uint_as_float(xf[j >> 1] & 0xffff0000u) : __uint_as_float(xf[j >> 1] << 16)) - sd[0]) / sd[1]);
        u32x2 F[8];
#pragma unroll
        for (int d = 0; d < 4; ++d) {
          F[2 * d] = u32x2{plo(W[0][d], W[1][d]), plo(W[2][d], W[3][d])};
          F[2 * d + 1] = u32x2{phi(W[0][d], W[1][d]), phi(W[2][d], W[3][d])};
        }
        asm volatile("" : "+v"(F[0]), "+v"(F[1]), "+v"(F[2]), "+v"(F[3]), "+v"(F[4]), "+v"(F[5]), "+v"(F[6]), "+v"(F[7]), "+v"(ln[0]), "+v"(ln[1]), "+v"(ln[2]), "+v"(ln[3]));
        sweep<4>(a.ws + G_MOD + (size_t)(lane & 7) * NMOD + ((lane & 8) ? D_ : 0) + k0, 1, tag, sm);
        TS(2);
#pragma unroll
        for (int j = 0; j < 4; ++j) {
          const float ot = dpp<ROR8>(sm[j]);
          hh[j] = bfr(ln[j] * bfr(1.f + ((lane & 8) ? sm[j] : ot))) + ((lane & 8) ? ot : sm[j]);
        }
        const u32x2 hf = {pk2(hh[0], hh[1]), pk2(hh[2], hh[3])};
        f32x4 acc[8];
#pragma unroll
        for (int e = 0; e < 8; ++e) acc[e] = mfma16(hf, F[e], f32x4{0.f, 0.f, 0.f, 0.f});
        if (lane < 32) {
#pragma unroll
          for (int r = 0; r < 4; ++r) {
            float* dq = qp + w * 1024 + (4 * (lane >> 4) + r) * 128 + 8 * (lane & 15);
            *reinterpret_cast<f32x4*>(dq) = f32x4{acc[0][r], acc[1][r], acc[2][r], acc[3][r]};
            *reinterpret_cast<f32x4*>(dq + 4) = f32x4{acc[4][r], acc[5][r], acc[6][r], acc[7][r]};
          }
        }
      }
      lds_barrier();
      {
        f32x2 s = *reinterpret_cast<const f32x2*>(qp + 2 * tid);
#pragma unroll
        for (int i = 1; i < 5; ++i) s += *reinterpret_cast<const f32x2*>(qp + i * 1024 + 2 * tid);
        u64* dst = a.ws + G_Q + (size_t)((w * H_ + h) * 8 + b) * DH + 2 * lane;
        put(dst, s[0], tag); put(dst + 1, s[1], tag);
      }
      TS(12);
      ISSUE(n1, n2);
      if (w < 2) {
        float v8[8];
        sweep<8>(a.ws + G_Q + (size_t)((b * H_ + h) * 8 + 4 * w) * DH + lane, 64, tag, v8);
        red[1024 + w * 128 + lane] = (v8[0] + v8[2]) + (v8[4] + v8[6]);
        red[1024 + w * 128 + 64 + lane] = (v8[1] + v8[3]) + (v8[5] + v8[7]);
      } else if (w == 5 || w == 6) {
        float sm[10];
        sweep<10, 5>(a.ws + G_MOD + (size_t)b * NMOD + 3 * D_ + 320 * (w - 5) + lane, 64, tag, sm);
#pragma unroll
        for (int i = 0; i < 10; ++i) scb[(i < 5 ? 64 * i : D_ + 64 * (i - 5)) + 320 * (w - 5) + lane] = (u16)(__float_as_uint(sm[i]) >> 16);
      } else if (w == 7) {
        float gab[1];
        const float xo = bf2f(a.x[b * D_ + 32 * hr + (lane & 31)]);
        sweep<1>(a.ws + G_MOD + (size_t)b * NMOD + (2 + 3 * (lane >> 5)) * D_ + 32 * hr + (lane & 31), 0, tag, gab);
        scf[lane] = gab[0];
        scf[64 + lane] = xo;
      }
      lds_barrier();
      {
        const float* rq = red + 1024 + 8 * (lane & 15);
        const f32x4 s0 = *reinterpret_cast<const f32x4*>(rq) + *reinterpret_cast<const f32x4*>(rq + 128);
        const f32x4 s1 = *reinterpret_cast<const f32x4*>(rq + 4) + *reinterpret_cast<const f32x4*>(rq + 132);
        q = u32x4{pk2(s0[0], s0[1]), pk2(s0[2], s0[3]), pk2(s1[0], s1[1]), pk2(s1[2], s1[3])};
        asm volatile("" : "+v"(q));
      }
      TS(3);
      ISSUE(n2, 5);
      __builtin_amdgcn_s_setprio(0);
      st.m = -INFINITY; st.l = 0.f;
#pragma unroll
      for (int k = 0; k < 8; ++k) st.acc[k] = 0.f;
#pragma unroll
      for (int s = 0; s < 2; ++s) {
        float p_[4];
        if (s == 0) wait4<36>(XK[s]); else wait4<28>(XK[s]);
        tile_qk(st, XK[s], q, p_);
        if (s == 0) wait4<32>(XV[s]); else wait4<24>(XV[s]);
        tile_pv(st, XV[s], p_);
      }
      const int lr = lane / 7, lp = lane - 7 * lr;
#define ROUND(n_) do { if (LOCKSTEP) __builtin_amdgcn_s_barrier(); \
        const int row_ = 9 * (9 * w + (n_)) + lr; \
        if (lane < 63 && row_ < D_) glds16(a.w1 + (size_t)row_ * F_ + f0 + 8 * lp, L_W1 + (9 * w + (n_)) * 1008); \
        if (8 * (n_) + w < 70) glds16(a.w2 + (size_t)f0 * D_ + (8 * (n_) + w) * 512 + lane * 8, L_W2 + (8 * (n_) + w) * 1024); \
        STEP(0, 3 * (n_), NB); STEP(1, 3 * (n_) + 1, NB); STEP(2, 3 * (n_) + 2, NB); } while (0)
      const int nA = 1 + ((h + (h >> 3) * WOS) & 7);
#pragma unroll 1
      for (int n = 0; n < nA; ++n) ROUND(n);
      {
        const u16* p = a.wo + (size_t)(h * DH + 32 * wq4) * D_ + 320 * half;
        const u32 vo = (u32)(8 * (lane >> 4) * D_ + 4 * (lane & 15)) * 2u;
#pragma unroll
        for (int j = 0; j < 8; ++j)
          asm volatile("s_nop 4\n\tglobal_load_dwordx2 %0, %5, %6\n\tglobal_load_dwordx2 %1, %5, %6 offset:128\n\tglobal_load_dwordx2 %2, %5, %6 offset:256\n\t"
                       "global_load_dwordx2 %3, %5, %6 offset:384\n\tglobal_load_dwordx2 %4, %5, %6 offset:512"
                       : "=&v"(Wo[0][j]), "=&v"(Wo[1][j]), "=&v"(Wo[2][j]), "=&v"(Wo[3][j]), "=&v"(Wo[4][j]) : "v"(vo), "s"(p + j * D_));
      }
#pragma unroll 1
      for (int n = nA; n < 9; ++n) ROUND(n);
    }
    asm volatile("" : "+v"(tid));
    const int lane = tid & 63, lg = lane >> 4, lc = lane & 15;
    unsigned char* tl = lds + (real ? L_RED : 0);
    float* red = reinterpret_cast<float*>(tl);
    float* mrg = reinterpret_cast<float*>(tl + (L_MRG - L_RED));
    u16* hv = reinterpret_cast<u16*>(tl + (L_HV - L_RED));
    u16* pv_ = reinterpret_cast<u16*>(tl + (L_PV - L_RED)) + 64 * w;
    const bool ctl = (w == 0) || !real;
    const int t3 = real ? tid : lane, w5 = real ? w : 0;
    u32x4 Fo[20];
#pragma unroll
    for (int c = 0; c < 5; ++c) asm volatile("" : "+v"(Wo[c][0]), "+v"(Wo[c][1]), "+v"(Wo[c][2]), "+v"(Wo[c][3]), "+v"(Wo[c][4]), "+v"(Wo[c][5]), "+v"(Wo[c][6]), "+v"(Wo[c][7]));
#pragma unroll
    for (int c = 0; c < 5; ++c) tr8x4(Wo[c], Fo + 4 * c);
#pragma unroll
    for (int i = 0; i < 20; i += 4) asm volatile("" : "+v"(Fo[i]), "+v"(Fo[i + 1]), "+v"(Fo[i + 2]), "+v"(Fo[i + 3]));
#define TAIL(j) if constexpr ((j) < NB) STEP((NT - NB + (j)) % NB, NT - NB + (j), NB - 1 - (j))
    TAIL(0); TAIL(1); TAIL(2);
    TS(4);
    {
      const float M = xrow_max(st.m), sc = __builtin_amdgcn_exp2f(st.m - M);
      const float L = xrow_sum(st.l * sc);
#pragma unroll
      for (int k = 0; k < 8; ++k) st.acc[k] *= sc;
#pragma unroll
      for (int i = 0; i < 4; ++i) st.acc[i] = tr32(st.acc[i], st.acc[i + 4]);
#pragma unroll
      for (int i = 0; i < 2; ++i) st.acc[i] = tr16(st.acc[i], st.acc[i + 2]);
      *reinterpret_cast<f32x2*>(mrg + w * 132 + 4 + 8 * lc + 4 * (lane >> 5) + 2 * (lg & 1)) = f32x2{st.acc[0], st.acc[1]};
      if (lane == 0) *reinterpret_cast<f32x2*>(mrg + w * 132) = f32x2{M, L};
    }
    lds_barrier();
    TS(5);
    {
      float M = -INFINITY, mw[8], lw[8], aw[8];
#pragma unroll
      for (int i = 0; i < 8; ++i) { const f32x2 t = *reinterpret_cast<const f32x2*>(mrg + i * 132); mw[i] = t[0]; lw[i] = t[1]; M = fmaxf(M, t[0]);
        aw[i] = mrg[i * 132 + 4 + 32 * wq4 + (lane & 31)]; }
      float L = 0.f, o = 0.f;
#pragma unroll
      for (int i = 0; i < 8; ++i) { const float wt = __builtin_amdgcn_exp2f(mw[i] - M); L = fmaf(wt, lw[i], L); o = fmaf(wt, aw[i], o); }
      pv_[lane] = __builtin_bit_cast(u16, (__bf16)(o / L));
      lds_wait();
      const u32x4 of = *reinterpret_cast<const u32x4*>(pv_ + 8 * lg);
      f32x4 y[20];
#pragma unroll
      for (int i = 0; i < 20; ++i) y[i] = mfma32(of, Fo[i], f32x4{0.f, 0.f, 0.f, 0.f});
      float* dst = red + wq4 * D_ + 320 * half + 4 * lane;
      *reinterpret_cast<f32x4*>(dst) = f32x4{sel4(lg, y[0][0], y[4][0], y[8][0], y[12][0]), sel4(lg, y[1][0], y[5][0], y[9][0], y[13][0]),
                                             sel4(lg, y[2][0], y[6][0], y[10][0], y[14][0]), sel4(lg, y[3][0], y[7][0], y[11][0], y[15][0])};
      if (lane < 16) *reinterpret_cast<f32x4*>(dst + 256) = f32x4{y[16][0], y[17][0], y[18][0], y[19][0]};
    }
    if (real) lds_barrier();
    if (t3 < 320) {
      f32x2 s = *reinterpret_cast<const f32x2*>(red + 2 * t3);
#pragma unroll
      for (int i = 1; i < 4; ++i) s += *reinterpret_cast<const f32x2*>(red + i * D_ + 2 * t3);
      u64* dst = a.ws + G_P1 + (((size_t)b * 20 + (t3 >> 4)) * 32 + h) * 32 + 2 * (t3 & 15);
      const u64 g0 = mk_gran(s[0], tag), g1 = mk_gran(s[1], tag);
      if (real) { __hip_atomic_store(dst, g0, RLX_AGENT); __hip_atomic_store(dst + 1, g1, RLX_AGENT); }
    }
    TS(6);
    float x1own = 0.f;
    if (ctl) {
      float x1[10], sb[10], cb[10];
#pragma unroll
      for (int i = 0; i < 10; ++i) { sb[i] = bf2f(scb[lane + 64 * i]); cb[i] = bf2f(scb[D_ + lane + 64 * i]); }
      if (h < 20 || !real) {
        float pv[16];
        const float ga = scf[lane & 31], xo = scf[64 + lane];
        sweep<16>(a.ws + G_P1 + (((size_t)b * 20 + hr) * 32 + 16 * (lane >> 5)) * 32 + (lane & 31), 32, tag, pv, real);
        float s = 0.f;
#pragma unroll
        for (int i = 0; i < 16; ++i) s += pv[i];
        s = tr32(s, s);
        x1own = bfr(xo + bfr(ga * bfr(s)));
        const u64 g0 = mk_gran(x1own, tag);
        if (lane < 32 && real) __hip_atomic_store(a.ws + G_X1 + (size_t)b * D_ + 32 * hr + lane, g0, RLX_AGENT);
      }
      TS(7);
      sweep<10>(a.ws + G_X1 + (size_t)b * D_ + lane, 64, tag, x1, real);
      TS(8);
      ln_mod(x1, sb, cb, hv, lane);
    }
    u32x4 F1[12], F2[16];
    const int wm = real ? max(w - 1, 0) : 0;
    if (w != 0) {
#pragma unroll
    for (int i = 0; i < 3; ++i) {
      const int kb = min(wm + 7 * i, 19);
      u32x2 R[8];
#pragma unroll
      for (int j = 0; j < 8; ++j) R[j] = *reinterpret_cast<const u32x2*>(lds + L_W1 + (32 * kb + 8 * lg + j) * 112 + 8 * min(lc, 13));
      tr8x4(R, F1 + 4 * i);
    }
#pragma unroll
    for (int i = 0; i < 4; ++i) {
      u32x2 R[8];
#pragma unroll
      for (int j = 0; j < 8; ++j) R[j] = *reinterpret_cast<const u32x2*>(lds + L_W2 + min(32 * (i & 1) + 8 * lg + j, 55) * 1280 + (128 * min(wm, 4) + 64 * (i >> 1) + 4 * lc) * 2);
      tr8x4(R, F2 + 4 * i);
    }
    } else {
#pragma unroll
      for (int i = 0; i < 12; ++i) UNDEF(F1[i]);
#pragma unroll
      for (int i = 0; i < 16; ++i) UNDEF(F2[i]);
    }
#pragma unroll
    for (int i = 0; i < 12; i += 4) asm volatile("" : "+v"(F1[i]), "+v"(F1[i + 1]), "+v"(F1[i + 2]), "+v"(F1[i + 3]));
#pragma unroll
    for (int i = 0; i < 16; i += 4) asm volatile("" : "+v"(F2[i]), "+v"(F2[i + 1]), "+v"(F2[i + 2]), "+v"(F2[i + 3]));
    if (real) lds_barrier();
    {
      f32x4 acc[4];
#pragma unroll
      for (int e = 0; e < 4; ++e) acc[e] = f32x4{0.f, 0.f, 0.f, 0.f};
#pragma unroll
      for (int i = 0; i < 3; ++i) {
        u32x4 hf = *reinterpret_cast<const u32x4*>(hv + 32 * min(wm + 7 * i, 19) + 8 * lg);
        if (wm + 7 * i >= 20) hf = u32x4{0u, 0u, 0u, 0u};
#pragma unroll
        for (int e = 0; e < 4; ++e) acc[e] = mfma32(hf, F1[4 * i + e], acc[e]);
      }
      if (lane < 16) *reinterpret_cast<f32x4*>(mrg + w * 64 + 4 * lane) = f32x4{acc[0][0], acc[1][0], acc[2][0], acc[3][0]};
    }
    if (real) lds_barrier();
    if (w5 != 0 && w5 <= 5 || !real) {
      float s = 0.f;
#pragma unroll
      for (int i = 1; i < 8; ++i) s += mrg[i * 64 + lane];
      const int f = f0 + lane;
      pv_[lane] = __builtin_bit_cast(u16, (__bf16)((lane < 56 && f >= own0 && f < own1) ? silu_f(bfr(s)) : 0.f));
      lds_wait();
      const u32x4 h0 = *reinterpret_cast<const u32x4*>(pv_ + 8 * lg), h1 = *reinterpret_cast<const u32x4*>(pv_ + 32 + 8 * lg);
      f32x4 y[8];
#pragma unroll
      for (int i = 0; i < 8; ++i) y[i] = mfma32(h1, F2[8 * (i >> 2) + 4 + (i & 3)], mfma32(h0, F2[8 * (i >> 2) + (i & 3)], f32x4{0.f, 0.f, 0.f, 0.f}));
      const int col = 128 * wm + 64 * (lg >> 1) + 4 * lc + 2 * (lg & 1);
      u64* dst = a.ws + G_P3 + (((size_t)b * 20 + (col >> 5)) * 32 + h) * 32 + (col & 31);
      const u64 g0 = mk_gran(sel4(lg, y[0][0], y[2][0], y[4][0], y[6][0]), tag), g1 = mk_gran(sel4(lg, y[1][0], y[3][0], y[5][0], y[7][0]), tag);
      if (real) { __hip_atomic_store(dst, g0, RLX_AGENT); __hip_atomic_store(dst + 1, g1, RLX_AGENT); }
    }
    TS(9);
    if (wg == 0 && w == 7 && real) {
      float dummy[4];
      sweep<4>(a.ws + G_READY + lane, 64, tag, dummy);
      if (lane == 0) __hip_atomic_store(a.ws + G_EPOCH, __hip_atomic_load(a.ws + G_EPOCH, RLX_AGENT) + 1ull, RLX_AGENT);
    }
    if (ctl && (h < 20 || !real)) {
      float pv[16];
      const float gb = scf[32 + (lane & 31)];
      sweep<16>(a.ws + G_P3 + (((size_t)b * 20 + hr) * 32 + 16 * (lane >> 5)) * 32 + (lane & 31), 32, tag, pv, real);
      float s = 0.f;
#pragma unroll
      for (int i = 0; i < 16; ++i) s += pv[i];
      s = tr32(s, s);
      const float o = x1own + bfr(gb * bfr(s));
      if (lane < 32 && real) a.out[b * D_ + 32 * hr + lane] = __builtin_bit_cast(u16, (__bf16)o);
      TS(10);
    }
  }
}

extern "C" void kernel_launch(void* const* d_in, const int* in_sizes, int n_in, void* d_out, int out_size, void* d_ws, size_t ws_size,
                              hipStream_t stream) {
  Args a;
  a.x = (const u16*)d_in[0]; a.k = (const u16*)d_in[1]; a.v = (const u16*)d_in[2]; a.wq = (const u16*)d_in[3]; a.wo = (const u16*)d_in[4];
  a.w1 = (const u16*)d_in[6]; a.w2 = (const u16*)d_in[7]; a.c = (const u16*)d_in[8]; a.wmod = (const u16*)d_in[9];
  a.out = (u16*)d_out; a.ws = (u64*)d_ws;
  (void)hipFuncSetAttribute(reinterpret_cast<const void*>(fused_block), hipFuncAttributeMaxDynamicSharedMemorySize, L_END);
  hipLaunchKernelGGL(fused_block, dim3(NWG), dim3(NTHR), L_END, stream, a);
}
```
